```python
import torch

def _softmax(x, axis=-1):
    return torch.softmax(x, dim=axis)
def _gelu(x):
    return torch.nn.functional.gelu(x, approximate="tanh")
def _silu(x):
    return torch.nn.functional.silu(x)
def _sigmoid(x):
    return torch.sigmoid(x)
def _layernorm(x, axis=-1, eps=1e-5):
    m = x.mean(dim=axis, keepdim=True)
    v = x.var(dim=axis, keepdim=True, unbiased=False)
    return (x - m) / torch.sqrt(v + eps)
def _scatter_add(tgt, idx, src, axis=0):
    return tgt.clone().index_add_(axis, idx, src)
def _segment_sum(src, seg, n):
    out = torch.zeros((n,) + src.shape[1:], dtype=src.dtype)
    return out.index_add_(0, seg, src)
def _pad(x, pw):
    flat = tuple(v for pair in reversed(pw) for v in pair)
    return torch.nn.functional.pad(x, flat)

kernel_name = 'concat_heads__sliding_block_cached__mha_block_cached__decode'

def setup_inputs():

    _w = (lambda a: (lambda t: t.to(torch.bfloat16) if t.is_floating_point() else t)(torch.as_tensor(a)))
    def _A_setup():
        B, Sq, D = 2, 1, 576
        Hq, Hkv, Sk, Dh = 32, 16, 8192, 128
        assert Hq % Hkv == 0, (Hq, Hkv)
        return {
            "x": _w((lambda *s: torch.randn(*s, dtype=torch.bfloat16) * 0.8)(B, Sq, D)),
            "k_cache": _w((lambda *s: torch.randn(*s, dtype=torch.bfloat16) * 1.6)(B, Hkv, Sk, Dh)),
            "v_cache": _w((lambda *s: torch.randn(*s, dtype=torch.bfloat16) * 0.2)(B, Hkv, Sk, Dh)),
            "wq": _w((lambda *s: torch.randn(*s, dtype=torch.bfloat16) * 1.6)(D, Hq * Dh) * (D ** -0.5)),
            "wo": _w((lambda *s: torch.randn(*s, dtype=torch.bfloat16) * 0.2)(Hq * Dh, D) * ((Hq * Dh) ** -0.5)),
        }
    _A_in = _A_setup()

    def _B_setup():
        B, Sq, D = 2, 1, 576
        H, Sk, Dh = 32, 8192, 128
        return {
            "x": _w((lambda *s: torch.randn(*s, dtype=torch.bfloat16) * 0.8)(B, Sq, D)),
            "k_cache": _w((lambda *s: torch.randn(*s, dtype=torch.bfloat16) * 1.6)(B, H, Sk, Dh)),
            "v_cache": _w((lambda *s: torch.randn(*s, dtype=torch.bfloat16) * 0.2)(B, H, Sk, Dh)),
            "wq": _w((lambda *s: torch.randn(*s, dtype=torch.bfloat16) * 1.6)(D, H * Dh) * (D ** -0.5)),
            "wo": _w((lambda *s: torch.randn(*s, dtype=torch.bfloat16) * 0.2)(H * Dh, D) * ((H * Dh) ** -0.5)),
        }
    _B_in = _B_setup()

    return {
        "A_x": _A_in["x"],
        "A_k_cache": _A_in["k_cache"],
        "A_v_cache": _A_in["v_cache"],
        "A_wq": _A_in["wq"],
        "A_wo": _A_in["wo"],
        "B_x": _B_in["x"],
        "B_k_cache": _B_in["k_cache"],
        "B_v_cache": _B_in["v_cache"],
        "B_wq": _B_in["wq"],
        "B_wo": _B_in["wo"],
    }

def reference(A_x, A_k_cache, A_v_cache, A_wq, A_wo, B_x, B_k_cache, B_v_cache, B_wq, B_wo):
    def _A_ref(x, k_cache, v_cache, wq, wo):
        B, Sq, D = x.shape
        Hq, Dh = 32, 128
        Hkv, Sk = k_cache.shape[1], k_cache.shape[2]
        W = min(4096, max(256, Sk // 4))
        rep = Hq // Hkv
        q = (lambda x, *d: x.permute(*d))((x @ wq).reshape(B, Sq, Hq, Dh), 0, 2, 1, 3)
        k = (lambda x, n, axis: torch.repeat_interleave(x, n, dim=axis))(k_cache[:, :, Sk - W:, :], rep, axis=1)
        v = (lambda x, n, axis: torch.repeat_interleave(x, n, dim=axis))(v_cache[:, :, Sk - W:, :], rep, axis=1)
        scores = torch.einsum("bhqd,bhkd->bhqk", q, k) * (Dh ** -0.5)
        p = _softmax(scores, axis=-1)
        o = torch.einsum("bhqk,bhkd->bhqd", p, v)
        return (lambda x, *d: x.permute(*d))(o, 0, 2, 1, 3).reshape(B, Sq, Hq * Dh) @ wo

    def _B_ref(x, k_cache, v_cache, wq, wo):
        B, Sq, D = x.shape
        H, Dh = 32, 128
        q = (lambda x, *d: x.permute(*d))((x @ wq).reshape(B, Sq, H, Dh), 0, 2, 1, 3)
        scores = torch.einsum("bhqd,bhkd->bhqk", q, k_cache) * (Dh ** -0.5)
        p = _softmax(scores, axis=-1)
        o = torch.einsum("bhqk,bhkd->bhqd", p, v_cache)
        return (lambda x, *d: x.permute(*d))(o, 0, 2, 1, 3).reshape(B, Sq, H * Dh) @ wo

    a = _A_ref(A_x, A_k_cache, A_v_cache, A_wq, A_wo)
    b = _B_ref(B_x, B_k_cache, B_v_cache, B_wq, B_wo)
    return torch.concatenate([a, b], axis=-1)
```

```cpp
#include <hip/hip_runtime.h>
#include <hip/hip_bf16.h>
#include <stdint.h>

typedef unsigned v4u __attribute__((ext_vector_type(4)));
typedef unsigned v2u __attribute__((ext_vector_type(2)));
typedef float f32x4 __attribute__((ext_vector_type(4)));
typedef __bf16 bf16x8 __attribute__((ext_vector_type(8)));
typedef __amdgpu_buffer_rsrc_t rsrc_t;
typedef unsigned long long u64;

#define KV_AUX 2

constexpr int DM = 576, DH = 128, NH = 32, SK = 8192, WIN = 2048, HKVA = 16;
constexpr int NWG = 256, NTHR = 512;
constexpr int AUX_SC1 = 16;
constexpr unsigned SPIN_CAP = 1u << 20;

constexpr unsigned WS_EPOCH = 0;
constexpr unsigned WS_RECB = 4096;
constexpr unsigned WS_RECA = WS_RECB + 2 * 48 * 128 * 64;
constexpr unsigned WS_END = WS_RECA + 2 * 48 * 16 * 64;

constexpr int L_QP = 0;
constexpr int L_QS = 2048;
constexpr int L_OB = 2304;
constexpr int L_ML = L_OB + 2048;
constexpr int L_YB = L_ML + 32;
constexpr int L_RD = L_YB + 8 * 5 * 256;
constexpr int L_FLOATS = L_RD + 128;

__device__ __forceinline__ rsrc_t mk_rsrc(const void* p, unsigned bytes) {
  return __builtin_amdgcn_make_buffer_rsrc(const_cast<void*>(p), (short)0, bytes, 0x00020000);
}
template <int AUX> __device__ __forceinline__ v4u ld16(rsrc_t r, int voff, int soff) {
  return __builtin_amdgcn_raw_buffer_load_b128(r, voff, soff, AUX);
}
__device__ __forceinline__ f32x4 mfma(v4u a, v4u b, f32x4 c) {
  return __builtin_amdgcn_mfma_f32_16x16x32_bf16(__builtin_bit_cast(bf16x8, a), __builtin_bit_cast(bf16x8, b), c, 0, 0, 0);
}
template <int CTRL> __device__ __forceinline__ float dppf(float x) {
  return __builtin_bit_cast(float, __builtin_amdgcn_mov_dpp(__builtin_bit_cast(int, x), CTRL, 0xf, 0xf, true));
}
template <int CTRL> __device__ __forceinline__ unsigned dppu(unsigned x) {
  return (unsigned)__builtin_amdgcn_mov_dpp((int)x, CTRL, 0xf, 0xf, true);
}
constexpr int BC0 = 0x00, BC1 = 0x55, BC2 = 0xAA, BC3 = 0xFF, ROR4 = 0x124, ROR8 = 0x128;
__device__ __forceinline__ float xg_max(float x) {
  auto s = __builtin_amdgcn_permlane16_swap(__float_as_uint(x), __float_as_uint(x), false, false);
  x = fmaxf(__uint_as_float(s[0]), __uint_as_float(s[1]));
  auto t = __builtin_amdgcn_permlane32_swap(__float_as_uint(x), __float_as_uint(x), false, false);
  return fmaxf(__uint_as_float(t[0]), __uint_as_float(t[1]));
}
__device__ __forceinline__ float xg_sum(float x) {
  auto s = __builtin_amdgcn_permlane16_swap(__float_as_uint(x), __float_as_uint(x), false, false);
  x = __uint_as_float(s[0]) + __uint_as_float(s[1]);
  auto t = __builtin_amdgcn_permlane32_swap(__float_as_uint(x), __float_as_uint(x), false, false);
  return __uint_as_float(t[0]) + __uint_as_float(t[1]);
}
__device__ __forceinline__ unsigned pk_bf16(float lo, float hi) {
  typedef __bf16 bf2 __attribute__((ext_vector_type(2)));
  typedef float f2 __attribute__((ext_vector_type(2)));
  f2 v = {lo, hi};
  return __builtin_bit_cast(unsigned, __builtin_convertvector(v, bf2));
}
__device__ __forceinline__ v4u diag(unsigned pp, const v4u& mask) {
  return v4u{pp & mask[0], pp & mask[1], pp & mask[2], pp & mask[3]};
}
#define SB() __builtin_amdgcn_sched_barrier(0)

constexpr float C2 = 0.088388347648318440f * 1.4426950408889634f;

struct St { float m, l; f32x4 acc; };

template <typename FK, typename FV>
__device__ __forceinline__ void tile_step(v4u (&K)[4], v4u (&V)[4], const v4u& qB, const v4u& mask, St& st, FK refillK, FV refillV) {
  f32x4 d[4];
#pragma unroll
  for (int a = 0; a < 4; ++a) d[a] = mfma(K[a], qB, f32x4{0.f, 0.f, 0.f, 0.f});
  SB();
  refillK();
  SB();
  float s[4];
#pragma unroll
  for (int a = 0; a < 4; ++a) s[a] = dppf<BC3>(d[a][3]) + (dppf<BC2>(d[a][2]) + (dppf<BC1>(d[a][1]) + dppf<BC0>(d[a][0])));
  float mx = fmaxf(fmaxf(s[0], s[1]), fmaxf(s[2], s[3]));
  mx = xg_max(mx);
  const float mn = fmaxf(st.m, mx);
  const float alpha = __builtin_amdgcn_exp2f((st.m - mn) * C2);
  const float nb = -mn * C2;
  float p[4];
#pragma unroll
  for (int a = 0; a < 4; ++a) p[a] = __builtin_amdgcn_exp2f(fmaf(s[a], C2, nb));
  st.l = fmaf(st.l, alpha, (p[0] + p[1]) + (p[2] + p[3]));
  st.m = mn;
  st.acc *= alpha;
#pragma unroll
  for (int a = 0; a < 4; ++a) st.acc = mfma(V[a], diag(pk_bf16(p[a], p[a]), mask), st.acc);
  SB();
  refillV();
  SB();
}

__device__ __forceinline__ void y_mfma(const v4u (&W)[18], const v4u (&A)[4], f32x4 (&Y)[4], f32x4& Ra, f32x4& Rb) {
#pragma unroll
  for (int cb = 0; cb < 4; ++cb) {
    Y[cb] = f32x4{0.f, 0.f, 0.f, 0.f};
#pragma unroll
    for (int rg = 0; rg < 4; ++rg) Y[cb] = mfma(A[rg], W[rg * 4 + cb], Y[cb]);
  }
  Ra = mfma(A[0], W[16], f32x4{0.f, 0.f, 0.f, 0.f});
  Ra = mfma(A[2], W[17], Ra);
  Rb = mfma(A[1], W[16], f32x4{0.f, 0.f, 0.f, 0.f});
  Rb = mfma(A[3], W[17], Rb);
}

__device__ __forceinline__ void load_wo(v4u (&W)[18], rsrc_t rw, int voM, int voR, int sbase) {
#pragma unroll
  for (int rg = 0; rg < 4; ++rg)
#pragma unroll
    for (int cb = 0; cb < 4; ++cb) W[rg * 4 + cb] = ld16<0>(rw, voM + cb * 256, sbase + rg * 4 * 1152);
  W[16] = ld16<0>(rw, voR, sbase);
  W[17] = ld16<0>(rw, voR, sbase + 8 * 1152);
}

template <bool IS_A>
__device__ __forceinline__ void produce(float* lds, const uint16_t* x, const uint16_t* kc, const uint16_t* vc, const uint16_t* wq,
                                        const uint16_t* wo, unsigned char* ws, int grp, int k, u64& tag_out) {
  const int tid = threadIdx.x, lane = tid & 63, w = __builtin_amdgcn_readfirstlane(tid >> 6);
  const int g = lane >> 4, c = lane & 15;
  int b, t0, ntile;
  constexpr int TSTR = IS_A ? 8 : 28;
  size_t slab;
  if (IS_A) {
    b = grp & 1;
    slab = ((size_t)(b * HKVA + (grp >> 1)) * SK + (SK - WIN)) * DH;
    t0 = w; ntile = 16;
  } else {
    b = (k < 3) ? 0 : (k > 3) ? 1 : (w >> 2);
    const int kk = (k > 3) ? k - 4 : k;
    t0 = (k == 3) ? 7 * (w & 3) + 3 : 7 * (w >> 1) + ((w & 1) ? 4 + kk : kk);
    ntile = (t0 < 8) ? 19 : 18;
    slab = (size_t)(b * NH + grp) * SK * DH;
  }
  constexpr unsigned SLAB_BYTES = (IS_A ? WIN : SK) * DH * 2;
  const rsrc_t rk = mk_rsrc(kc + slab, SLAB_BYTES), rv = mk_rsrc(vc + slab, SLAB_BYTES);
  const rsrc_t rq = mk_rsrc(wq, DM * NH * DH * 2), rw = mk_rsrc(wo, DM * NH * DH * 2), rx = mk_rsrc(x, 2 * DM * 2);
  const rsrc_t rws = mk_rsrc(ws, WS_END);
  int voK = ((lane >> 2) & 3) * 256 + (4 * g + (lane & 3)) * 16, voV = lane * 16;
  const int head0 = IS_A ? (grp >> 1) * 2 : grp;

  v4u K[4][4], V[4][4], W[18];
#define LOADK(S, T) _Pragma("unroll") for (int a_ = 0; a_ < 4; ++a_) K[S][a_] = ld16<KV_AUX>(rk, voK + a_ * 1024, (T) * 4096)
#define LOADV(S, T) _Pragma("unroll") for (int a_ = 0; a_ < 4; ++a_) V[S][a_] = ld16<KV_AUX>(rv, voV + a_ * 1024, (T) * 4096)
  LOADK(0, t0); LOADV(0, t0);
  SB();
  const v2u ep = __builtin_amdgcn_raw_buffer_load_b64(rws, WS_EPOCH, 0, AUX_SC1);
  const int xo = ((IS_A ? b : (c >> 3)) * DM + 72 * w + 18 * g) * 2;
  const v4u xa = ld16<0>(rx, xo, 0), xb = ld16<0>(rx, xo + 16, 0);
  const unsigned xc = __builtin_amdgcn_raw_buffer_load_b32(rx, xo + 32, 0, 0);
  const int voQ = 18 * g * (NH * DH * 2) + c * 16, soQ = 72 * w * (NH * DH * 2) + head0 * 256;
#pragma unroll
  for (int i = 0; i < 18; ++i) W[i] = ld16<0>(rq, voQ, soQ + i * (NH * DH * 2));
  v4u W1[18];
  if (IS_A) {
#pragma unroll
    for (int i = 0; i < 18; ++i) W1[i] = ld16<0>(rq, voQ + 256, soQ + i * (NH * DH * 2));
  }
  SB();
  LOADK(1, t0 + TSTR); LOADV(1, t0 + TSTR);
  if (!IS_A) {
    LOADK(2, t0 + 2 * TSTR); LOADV(2, t0 + 2 * TSTR);
    LOADK(3, t0 + 3 * TSTR); LOADV(3, t0 + 3 * TSTR);
  }
  SB();
  v4u mask;
#pragma unroll
  for (int d = 0; d < 4; ++d) mask[d] = (((c & 7) >> 1) == d) ? ((c & 1) ? 0xffff0000u : 0x0000ffffu) : 0u;
  {
    const unsigned xr[9] = {xa[0], xa[1], xa[2], xa[3], xb[0], xb[1], xb[2], xb[3], xc};
    f32x4 q0 = {0.f, 0.f, 0.f, 0.f}, q1 = {0.f, 0.f, 0.f, 0.f};
#pragma unroll
    for (int i = 0; i < 18; ++i) {
      const unsigned pp = __builtin_amdgcn_perm(xr[i >> 1], xr[i >> 1], (i & 1) ? 0x03020302u : 0x01000100u);
      const v4u A = diag(pp, mask);
      q0 = mfma(A, W[i], q0);
      if (IS_A) q1 = mfma(A, W1[i], q1);
    }
    if (IS_A && lane >= 32) q0 = q1;
    *reinterpret_cast<f32x4*>(lds + L_QP + w * 256 + lane * 4) = q0;
  }
  SB();
  if (IS_A) {
    LOADK(2, t0 + 2 * TSTR); LOADV(2, t0 + 2 * TSTR);
    LOADK(3, t0 + 3 * TSTR); LOADV(3, t0 + 3 * TSTR);
  }
  const int voM = g * 1152 + c * 16, voR = (4 * (c >> 3) + g) * 1152 + 1024 + (c & 7) * 16;
  const int soW = (head0 * 128 + 16 * w) * 1152;
  load_wo(W, rw, voM, voR, soW);
  SB();
  __syncthreads();
  if (tid < 128) {
    float s0 = 0.f, s1 = 0.f;
#pragma unroll
    for (int ww = 0; ww < 8; ++ww) {
      const float2 v = *reinterpret_cast<const float2*>(lds + L_QP + ww * 256 + 2 * tid);
      s0 += v.x; s1 += v.y;
    }
    const int sl = tid >> 1, sg = sl >> 4, sc = sl & 15;
    reinterpret_cast<unsigned*>(lds + L_QS)[(sg >> 1) * 64 + sc * 4 + (sg & 1) * 2 + (tid & 1)] = pk_bf16(s0, s1);
  }
  __syncthreads();
  const v4u qB = *reinterpret_cast<const v4u*>(lds + L_QS + (IS_A ? (c >> 3) : b) * 64 + (4 * g + (c & 3)) * 4);
  const u64 tag = (((u64)__builtin_amdgcn_readfirstlane(ep[1]) << 32) | (u64)__builtin_amdgcn_readfirstlane(ep[0])) + 0x0040100401ull;
  tag_out = tag;

  St st; st.m = -INFINITY; st.l = 0.f; st.acc = f32x4{0.f, 0.f, 0.f, 0.f};
  if (IS_A) {
    int tn = t0 + 4 * TSTR;
#pragma unroll 1
    for (int it = 0; it < 3; ++it) {
      asm volatile("" : "+v"(voK), "+v"(voV));
#pragma unroll
      for (int s = 0; s < 4; ++s) {
        tile_step(K[s], V[s], qB, mask, st, [&]() { LOADK(s, tn); }, [&]() { LOADV(s, tn); });
        tn += TSTR;
      }
    }
    const int soW1 = soW + 128 * 1152;
#define LW1(I) W1[I] = ld16<0>(rw, voM + ((I) & 3) * 256, soW1 + ((I) >> 2) * 4 * 1152)
    tile_step(K[0], V[0], qB, mask, st, [&]() { LW1(0); LW1(1); LW1(2); LW1(3); }, [&]() { LW1(4); LW1(5); LW1(6); LW1(7); });
    tile_step(K[1], V[1], qB, mask, st, [&]() { LW1(8); LW1(9); LW1(10); LW1(11); }, [&]() { LW1(12); LW1(13); LW1(14); LW1(15); });
    tile_step(K[2], V[2], qB, mask, st, [&]() { W1[16] = ld16<0>(rw, voR, soW1); W1[17] = ld16<0>(rw, voR, soW1 + 8 * 1152); }, [&]() {});
    tile_step(K[3], V[3], qB, mask, st, [&]() {}, [&]() {});
#undef LW1
  } else {
    int tn = t0 + 4 * TSTR;
#pragma unroll 1
    for (int it = 0; it < 4; ++it) {
      asm volatile("" : "+v"(voK), "+v"(voV));
#pragma unroll
      for (int s = 0; s < 4; ++s) {
        tile_step(K[s], V[s], qB, mask, st, [&]() { LOADK(s, tn); }, [&]() { LOADV(s, tn); });
        tn += TSTR;
      }
    }
    tile_step(K[0], V[0], qB, mask, st, [&]() {}, [&]() {});
    tile_step(K[1], V[1], qB, mask, st, [&]() {}, [&]() {});
    if (ntile == 19) tile_step(K[2], V[2], qB, mask, st, [&]() {}, [&]() {});
  }
#undef LOADK
#undef LOADV

  const float lt = xg_sum(st.l);
#pragma unroll
  for (int r = 0; r < 4; ++r) lds[L_OB + w * 256 + r * 64 + lane] = st.acc[r];
  if ((lane & 55) == 0) { lds[L_ML + (lane >> 3) * 8 + w] = st.m * C2; lds[L_ML + 16 + (lane >> 3) * 8 + w] = lt; }
  __syncthreads();
  const bool two = IS_A || (k == 3);
  const int vec = c >> 3;
  v4u A[4];
  {
    const int hsel = IS_A ? vec : 0;
    const f32x4 m0 = *reinterpret_cast<const f32x4*>(lds + L_ML + hsel * 8), m1 = *reinterpret_cast<const f32x4*>(lds + L_ML + hsel * 8 + 4);
    float mm[8] = {m0[0], m0[1], m0[2], m0[3], m1[0], m1[1], m1[2], m1[3]};
    if (!IS_A && k == 3) {
#pragma unroll
      for (int ww = 0; ww < 8; ++ww) mm[ww] = ((ww >> 2) == vec) ? mm[ww] : -INFINITY;
    }
    float M = mm[0];
#pragma unroll
    for (int ww = 1; ww < 8; ++ww) M = fmaxf(M, mm[ww]);
    const int rs = c & 3;
    const int src = (2 * (w & 1) + (rs >> 1)) * 64 + (w >> 1) * 16 + hsel * 8 + 4 * (rs & 1) + g;
    float o = 0.f, L = 0.f;
    f32x4 l0, l1;
    if (IS_A) { l0 = *reinterpret_cast<const f32x4*>(lds + L_ML + 16 + hsel * 8); l1 = *reinterpret_cast<const f32x4*>(lds + L_ML + 16 + hsel * 8 + 4); }
#pragma unroll
    for (int ww = 0; ww < 8; ++ww) {
      const float e = __builtin_amdgcn_exp2f(mm[ww] - M);
      o = fmaf(e, lds[L_OB + ww * 256 + src], o);
      if (IS_A) L = fmaf(e, ww < 4 ? l0[ww & 3] : l1[ww & 3], L);
    }
    if (IS_A) o = o / L;
    const unsigned pp = pk_bf16(o, o);
    A[0] = diag(dppu<BC0>(pp), mask); A[1] = diag(dppu<BC1>(pp), mask); A[2] = diag(dppu<BC2>(pp), mask); A[3] = diag(dppu<BC3>(pp), mask);
  }
  f32x4 Y[4], Ra, Rb;
  y_mfma(W, A, Y, Ra, Rb);
  if (IS_A) {
    f32x4 Y1[4], Ra1, Rb1;
    y_mfma(W1, A, Y1, Ra1, Rb1);
    if (lane >= 32) { Y[0] = Y1[0]; Y[1] = Y1[1]; Y[2] = Y1[2]; Y[3] = Y1[3]; Ra = Ra1; Rb = Rb1; }
  }
  f32x4 R;
#pragma unroll
  for (int r = 0; r < 4; ++r) R[r] = Ra[r] + dppf<ROR8>(Rb[r]);
  {
    f32x4* yb = reinterpret_cast<f32x4*>(lds + L_YB) + w * 5 * 64 + lane;
    yb[0] = Y[0]; yb[64] = Y[1]; yb[128] = Y[2]; yb[192] = Y[3]; yb[256] = R;
  }
  __syncthreads();
  {
    const int pv = tid >> 8, tt = tid & 255;
    const bool act = tt < 192 && (pv == 0 || (!IS_A && k == 3));
    if (act) {
      const int r = tt >> 2, q = tt & 3;
      v4u gr;
      if (q == 0) {
        float M = -INFINITY, L = 0.f;
        if (!IS_A) {
          const int w0 = (k == 3) ? 4 * pv : 0, w1 = (k == 3) ? w0 + 4 : 8;
          for (int ww = w0; ww < w1; ++ww) M = fmaxf(M, lds[L_ML + ww]);
          for (int ww = w0; ww < w1; ++ww) L = fmaf(__builtin_amdgcn_exp2f(lds[L_ML + ww] - M), lds[L_ML + 16 + ww], L);
        }
        const unsigned mu = __float_as_uint(M), lu = __float_as_uint(L);
        gr = v4u{mu & ~0x3ffu, mu << 22, lu & ~0x3ffu, lu << 22};
      } else {
        const int gq = 3 * r + q - 1, chunk = gq >> 1, jh = gq & 1;
        const f32x4* yb = reinterpret_cast<const f32x4*>(lds + L_YB) + (chunk >> 4) * 64 + jh * 16 + (chunk & 15);
        f32x4 s = {0.f, 0.f, 0.f, 0.f};
        if (IS_A) {
#pragma unroll
          for (int ww = 0; ww < 8; ++ww) s += yb[ww * 320] + yb[ww * 320 + 32];
        } else {
#pragma unroll
          for (int ww = 0; ww < 8; ++ww) s += yb[ww * 320 + pv * 32];
        }
#pragma unroll
        for (int i = 0; i < 4; ++i) gr[i] = (__float_as_uint(s[i]) + 0x200u) & ~0x3ffu;
      }
#pragma unroll
      for (int i = 0; i < 4; ++i) gr[i] |= (unsigned)(tag >> (10 * i)) & 0x3ffu;
      unsigned off;
      if (IS_A) off = WS_RECA + (unsigned)(((grp & 1) * 48 + r) * 16 + (grp >> 1)) * 64 + q * 16;
      else {
        const int bb = (k < 3) ? 0 : (k > 3) ? 1 : pv;
        off = WS_RECB + (unsigned)((bb * 48 + r) * 128 + 4 * grp + (k - 3 * bb)) * 64 + q * 16;
      }
      __builtin_amdgcn_raw_buffer_store_b128(gr, rws, (int)off, 0, AUX_SC1);
    }
  }
  (void)two;
}

__device__ __forceinline__ v4u poll_granule(rsrc_t rws, unsigned off, u64 tag) {
  const unsigned t0 = (unsigned)tag & 0x3ffu, t1 = (unsigned)(tag >> 10) & 0x3ffu, t2 = (unsigned)(tag >> 20) & 0x3ffu, t3 = (unsigned)(tag >> 30) & 0x3ffu;
  v4u gv = {0u, 0u, 0u, 0u};
  bool ok = false;
  for (unsigned spins = 0; spins < SPIN_CAP; ++spins) {
    if (!ok) {
      gv = ld16<AUX_SC1>(rws, (int)off, 0);
      ok = (((gv[0] ^ t0) | (gv[1] ^ t1) | (gv[2] ^ t2) | (gv[3] ^ t3)) & 0x3ffu) == 0u;
    }
    if (__all(ok)) break;
    __builtin_amdgcn_s_sleep(2);
    asm volatile("" ::: "memory");
  }
  return gv;
}

__global__ void __launch_bounds__(NTHR, 2)
fused_decode_kernel(const uint16_t* __restrict__ ax, const uint16_t* __restrict__ ak, const uint16_t* __restrict__ av,
                    const uint16_t* __restrict__ awq, const uint16_t* __restrict__ awo,
                    const uint16_t* __restrict__ bx, const uint16_t* __restrict__ bk, const uint16_t* __restrict__ bv,
                    const uint16_t* __restrict__ bwq, const uint16_t* __restrict__ bwo,
                    uint16_t* __restrict__ out, unsigned char* __restrict__ ws) {
  __shared__ __attribute__((aligned(16))) float lds[L_FLOATS];
  const int bid = blockIdx.x;
  const int slot = bid >> 3, grp = (bid & 7) * 4 + (slot >> 3), mem = slot & 7;
  u64 tag;
  if (mem == 0) produce<true>(lds, ax, ak, av, awq, awo, ws, grp, 0, tag);
  else produce<false>(lds, bx, bk, bv, bwq, bwo, ws, grp, mem - 1, tag);

  if (bid > 108) return;
  const int tid = threadIdx.x, lane = tid & 63, w = __builtin_amdgcn_readfirstlane(tid >> 6), q = lane & 3;
  const rsrc_t rws = mk_rsrc(ws, WS_END);
  const rsrc_t ro = mk_rsrc(out, 2 * 2 * DM * 2);
  if (bid < 96) {
    const v4u gv = poll_granule(rws, WS_RECB + (unsigned)bid * 8192 + tid * 16, tag);
    const float m = dppf<BC0>(__uint_as_float((gv[0] & ~0x3ffu) | (gv[1] >> 22)));
    const float l = dppf<BC0>(__uint_as_float((gv[2] & ~0x3ffu) | (gv[3] >> 22)));
    float M = fmaxf(m, dppf<ROR4>(m)); M = fmaxf(M, dppf<ROR8>(M));
    const float e = __builtin_amdgcn_exp2f(m - M);
    float L = l * e; L += dppf<ROR8>(L); L += dppf<ROR4>(L);
    const float wg = e / L;
    f32x4 v;
#pragma unroll
    for (int i = 0; i < 4; ++i) {
      float t = (q == 0) ? 0.f : __uint_as_float(gv[i] & ~0x3ffu) * wg;
      t += dppf<ROR8>(t); t += dppf<ROR4>(t);
      v[i] = xg_sum(t);
    }
    if (lane < 4) *reinterpret_cast<f32x4*>(lds + L_RD + w * 16 + lane * 4) = v;
    __syncthreads();
    if (tid < 6) {
      float s0 = 0.f, s1 = 0.f;
#pragma unroll
      for (int ww = 0; ww < 8; ++ww) { s0 += lds[L_RD + ww * 16 + 4 + 2 * tid]; s1 += lds[L_RD + ww * 16 + 5 + 2 * tid]; }
      const int bb = bid / 48, r = bid % 48;
      __builtin_amdgcn_raw_buffer_store_b32(pk_bf16(s0, s1), ro, (bb * 2 * DM + DM + 12 * r + 2 * tid) * 2, 0, 0);
    }
  } else if (bid < 108) {
    const int row = (bid - 96) * 8 + w;
    const v4u gv = poll_granule(rws, WS_RECA + (unsigned)row * 1024 + lane * 16, tag);
    float v[4];
#pragma unroll
    for (int i = 0; i < 4; ++i) {
      float t = (q == 0) ? 0.f : __uint_as_float(gv[i] & ~0x3ffu);
      t += dppf<ROR8>(t); t += dppf<ROR4>(t);
      v[i] = xg_sum(t);
    }
    if (lane >= 1 && lane < 4) {
      const int bb = row / 48, r = row % 48;
      __builtin_amdgcn_raw_buffer_store_b64(v2u{pk_bf16(v[0], v[1]), pk_bf16(v[2], v[3])}, ro, (bb * 2 * DM + 12 * r + 4 * (lane - 1)) * 2, 0, 0);
    }
  } else {
    if (tid < 320) {
      unsigned off;
      if (tid < 256) off = WS_RECB + (unsigned)((tid >> 7) * 48 * 128 + (tid & 127)) * 64;
      else off = WS_RECA + (unsigned)((((tid - 256) >> 4) & 1) * 48 * 16 + (tid & 15)) * 64;
      (void)poll_granule(rws, off, tag);
    }
    __syncthreads();
    if (tid == 0) __builtin_amdgcn_raw_buffer_store_b64(v2u{(unsigned)tag, (unsigned)(tag >> 32)}, rws, WS_EPOCH, 0, AUX_SC1);
  }
}

extern "C" void kernel_launch(void* const* d_in, const int* in_sizes, int n_in, void* d_out, int out_size, void* d_ws, size_t ws_size,
                              hipStream_t stream) {
  (void)in_sizes; (void)n_in; (void)out_size; (void)ws_size;
  fused_decode_kernel<<<NWG, NTHR, 0, stream>>>(
      (const uint16_t*)d_in[0], (const uint16_t*)d_in[1], (const uint16_t*)d_in[2], (const uint16_t*)d_in[3], (const uint16_t*)d_in[4],
      (const uint16_t*)d_in[5], (const uint16_t*)d_in[6], (const uint16_t*)d_in[7], (const uint16_t*)d_in[8], (const uint16_t*)d_in[9],
      (uint16_t*)d_out, (unsigned char*)d_ws);
}
```

```cpp
#include <hip/hip_runtime.h>
#include <hip/hip_bf16.h>
#include <stdint.h>

typedef unsigned v4u __attribute__((ext_vector_type(4)));
typedef unsigned v2u __attribute__((ext_vector_type(2)));
typedef float f32x4 __attribute__((ext_vector_type(4)));
typedef __bf16 bf16x8 __attribute__((ext_vector_type(8)));
typedef __amdgpu_buffer_rsrc_t rsrc_t;
typedef unsigned long long u64;

constexpr int DM = 576, DH = 128, NH = 32, SK = 8192, WIN = 2048, HKVA = 16;
constexpr int NWG = 256, NTHR = 512;
constexpr int AUX_NT = 2, AUX_SC1 = 16;
#define POLL_AUX AUX_SC1
#define PUB_AUX AUX_SC1
#define WS_BASE 0
constexpr unsigned SPIN_CAP = 1u << 18;

constexpr unsigned WS_EPOCH = 0;
constexpr unsigned WS_CK = 4096;
constexpr unsigned WS_QP = 8192;
constexpr unsigned WS_ML = WS_QP + 2 * 2 * 32 * 2 * 1024;
constexpr unsigned WS_OB = WS_ML + 32 * 3 * 1024;
constexpr unsigned WS_Y = WS_OB + 2 * 32 * 8192;
constexpr unsigned WS_END = WS_Y + 2 * 2 * 18 * 4096;
constexpr int OOB = 0x40000000;

constexpr int L_QS = 0;
constexpr int L_CNT = 256;
constexpr int L_NB = 272;
constexpr int L_SCR = 288;
constexpr int L_QH = L_SCR + 64;
constexpr int L_OB = L_QH + 1024 + 96;
constexpr int L_YB = L_OB + 3 * 2048;
constexpr int L_SC = L_YB + 4 * 2048;
constexpr int L_FLOATS = L_SC + 8 * 18 * 128;

__device__ __forceinline__ rsrc_t mk_rsrc(const void* p, unsigned bytes) {
  return __builtin_amdgcn_make_buffer_rsrc(const_cast<void*>(p), (short)0, bytes, 0x00020000);
}
template <int AUX> __device__ __forceinline__ v4u ld16(rsrc_t r, int voff, int soff) {
  return __builtin_amdgcn_raw_buffer_load_b128(r, voff, soff, AUX);
}
__device__ __forceinline__ f32x4 mfma(v4u a, v4u b, f32x4 c) {
  return __builtin_amdgcn_mfma_f32_16x16x32_bf16(__builtin_bit_cast(bf16x8, a), __builtin_bit_cast(bf16x8, b), c, 0, 0, 0);
}
template <int CTRL> __device__ __forceinline__ float dppf(float x) {
  return __builtin_bit_cast(float, __builtin_amdgcn_mov_dpp(__builtin_bit_cast(int, x), CTRL, 0xf, 0xf, true));
}
constexpr int BC0 = 0x00, BC1 = 0x55, BC2 = 0xAA, BC3 = 0xFF, ROR4 = 0x124, ROR8 = 0x128, XOR1 = 0xB1, XOR2 = 0x4E, HMIR = 0x141;
__device__ __forceinline__ float xg_max(float x) {
  auto s = __builtin_amdgcn_permlane16_swap(__float_as_uint(x), __float_as_uint(x), false, false);
  x = fmaxf(__uint_as_float(s[0]), __uint_as_float(s[1]));
  auto t = __builtin_amdgcn_permlane32_swap(__float_as_uint(x), __float_as_uint(x), false, false);
  return fmaxf(__uint_as_float(t[0]), __uint_as_float(t[1]));
}
__device__ __forceinline__ float x32_sum(float x) {
  auto t = __builtin_amdgcn_permlane32_swap(__float_as_uint(x), __float_as_uint(x), false, false);
  return __uint_as_float(t[0]) + __uint_as_float(t[1]);
}
__device__ __forceinline__ float xg_sum(float x) {
  auto s = __builtin_amdgcn_permlane16_swap(__float_as_uint(x), __float_as_uint(x), false, false);
  return x32_sum(__uint_as_float(s[0]) + __uint_as_float(s[1]));
}
__device__ __forceinline__ float r8_sum(float x) { x += dppf<XOR1>(x); x += dppf<XOR2>(x); return x + dppf<HMIR>(x); }
__device__ __forceinline__ float r8_max(float x) { x = fmaxf(x, dppf<XOR1>(x)); x = fmaxf(x, dppf<XOR2>(x)); return fmaxf(x, dppf<HMIR>(x)); }
__device__ __forceinline__ unsigned pk_bf16(float lo, float hi) {
  typedef __bf16 bf2 __attribute__((ext_vector_type(2)));
  typedef float f2 __attribute__((ext_vector_type(2)));
  f2 v = {lo, hi};
  return __builtin_bit_cast(unsigned, __builtin_convertvector(v, bf2));
}
__device__ __forceinline__ v4u diag(unsigned pp, const v4u& mask) {
  return v4u{pp & mask[0], pp & mask[1], pp & mask[2], pp & mask[3]};
}
__device__ __forceinline__ float lo_f(unsigned u) { return __uint_as_float(u << 16); }
__device__ __forceinline__ float hi_f(unsigned u) { return __uint_as_float(u & 0xffff0000u); }
#define SB() __builtin_amdgcn_sched_barrier(0)
#define STAMP(i) do {} while (0)

constexpr float SCALE = 0.088388347648318440f, LOG2E = 1.4426950408889634f;

__device__ __forceinline__ void k_step(const v4u (&R)[4], const v4u& qB, float& m, float& l, v2u* sc) {
  f32x4 d[4];
#pragma unroll
  for (int a = 0; a < 4; ++a) d[a] = mfma(R[a], qB, f32x4{0.f, 0.f, 0.f, 0.f});
  float s[4];
#pragma unroll
  for (int a = 0; a < 4; ++a) s[a] = dppf<BC3>(d[a][3]) + (dppf<BC2>(d[a][2]) + (dppf<BC1>(d[a][1]) + dppf<BC0>(d[a][0])));
  const unsigned u0 = pk_bf16(s[0], s[1]), u1 = pk_bf16(s[2], s[3]);
  const unsigned w0 = pk_bf16(lo_f(u0) * SCALE, hi_f(u0) * SCALE), w1 = pk_bf16(lo_f(u1) * SCALE, hi_f(u1) * SCALE);
  *sc = v2u{w0, w1};
  s[0] = lo_f(w0); s[1] = hi_f(w0); s[2] = lo_f(w1); s[3] = hi_f(w1);
  const float mn = fmaxf(fmaxf(m, fmaxf(s[0], s[1])), fmaxf(s[2], s[3]));
  float e = __builtin_amdgcn_exp2f((m - mn) * LOG2E) * l;
#pragma unroll
  for (int a = 0; a < 4; ++a) e += __builtin_amdgcn_exp2f((s[a] - mn) * LOG2E);
  l = e; m = mn;
  SB();
}
__device__ __forceinline__ void v_step(const v4u (&R)[4], const v4u& mask, const float2& nb, f32x4& acc, const v2u* sc) {
  const v2u w = *sc;
  const float s[4] = {lo_f(w[0]), hi_f(w[0]), lo_f(w[1]), hi_f(w[1])};
  v4u P[4];
#pragma unroll
  for (int a = 0; a < 4; ++a) { const float p = __builtin_amdgcn_exp2f(fmaf(s[a] - nb.x, LOG2E, nb.y)); P[a] = diag(pk_bf16(p, p), mask); }
  SB();
#pragma unroll
  for (int a = 0; a < 4; ++a) acc = mfma(R[a], P[a], acc);
  SB();
}

__device__ __forceinline__ f32x4 y_half(const v4u& W0, const v4u& W1, const v4u (&A)[4]) {
  f32x4 Ra = mfma(A[0], W0, f32x4{0.f, 0.f, 0.f, 0.f});
  f32x4 Rb = mfma(A[1], W0, f32x4{0.f, 0.f, 0.f, 0.f});
  Ra = mfma(A[2], W1, Ra);
  Rb = mfma(A[3], W1, Rb);
  f32x4 R;
#pragma unroll
  for (int r = 0; r < 4; ++r) R[r] = Ra[r] + dppf<ROR8>(Rb[r]);
  return R;
}

template <int N> __device__ __forceinline__ void load_granules(rsrc_t rws, unsigned off, unsigned stride, v4u (&gv)[N]) {
#pragma unroll
  for (int i = 0; i < N; ++i) gv[i] = ld16<POLL_AUX>(rws, (int)off, (int)(i * stride));
}
template <int N> __device__ __forceinline__ bool tags_ok(const v4u (&gv)[N], u64 tag) {
  const unsigned t0 = (unsigned)tag, t1 = (unsigned)(tag >> 10), t2 = (unsigned)(tag >> 20), t3 = (unsigned)(tag >> 30);
  unsigned bad = 0u;
#pragma unroll
  for (int i = 0; i < N; ++i) bad |= (gv[i][0] ^ t0) | (gv[i][1] ^ t1) | (gv[i][2] ^ t2) | (gv[i][3] ^ t3);
  return __all((bad & 0x3ffu) == 0u);
}
template <int N>
__device__ __forceinline__ bool poll_granules(rsrc_t rws, unsigned off, unsigned stride, u64 tag, v4u (&gv)[N], unsigned tries = SPIN_CAP) {
#pragma unroll 1
  for (;;) {
    asm volatile("" : "+v"(off) :: "memory");
    load_granules<N>(rws, off, stride, gv);
    if (tags_ok<N>(gv, tag)) return true;
    if (--tries == 0u) return false;
  }
}
template <int N>
__device__ __forceinline__ bool check_granules(rsrc_t rws, unsigned off, unsigned stride, u64 tag, v4u (&gv)[N], unsigned retries) {
  bool ok = tags_ok<N>(gv, tag);
  if (!ok && retries) ok = poll_granules<N>(rws, off, stride, tag, gv, retries);
  return ok;
}
__device__ __forceinline__ float untag(unsigned u) { return __uint_as_float(u & ~0x3ffu); }
__device__ __forceinline__ void publish(rsrc_t rws, int voff, int soff, const f32x4& s, u64 tag) {
  v4u gr;
#pragma unroll
  for (int i = 0; i < 4; ++i) gr[i] = ((__float_as_uint(s[i]) + 0x200u) & ~0x3ffu) | ((unsigned)(tag >> (10 * i)) & 0x3ffu);
  __builtin_amdgcn_raw_buffer_store_b128(gr, rws, voff, soff, PUB_AUX);
}

#define PACE_TBS 7.85
#define LEAD_TICKS 0
#define XSHIFT 4
#define LAT_NOM 85
#define GAP1_SLOTS 0.5
#define CATCH_MAX 50
#define CATCH_MIN 30
#define WOFF_TICKS 0
#define WQ_STEP 1.2
#define UNIT0_SLOTS 10.5
#define PDELAY2 315
#define PDELAY 140
#define CODE_KB 17
#define W_SCALE 1.0
#define GAP_SLOTS 0.5
#define SHIFT_MUL 502
#define SHIFT_A 32
#define DFLG 0
constexpr unsigned S_FP = (unsigned)(4096.0 * NWG / (PACE_TBS * 1e12) * 1e8 * 256.0);
constexpr unsigned GAP_FP = (unsigned)(GAP_SLOTS * S_FP);
__device__ __forceinline__ void wait_until(unsigned ts, unsigned lim_fp) {
  const int lim = (int)(lim_fp >> 8);
  while ((int)((unsigned)__builtin_amdgcn_s_memrealtime() - ts) < lim) __builtin_amdgcn_s_sleep(1);
}
__device__ __forceinline__ unsigned lds_arrive(float* lds, int idx, int lane) {
  unsigned old;
  const unsigned addr = (unsigned)(size_t)(__attribute__((address_space(3))) float*)(lds + (lane == 0 ? L_CNT + idx : L_SCR + lane));
  asm volatile("ds_add_rtn_u32 %0, %1, %2\n\ts_waitcnt lgkmcnt(0)" : "=v"(old) : "v"(addr), "v"(1u) : "memory");
  return __builtin_amdgcn_readfirstlane(old);
}
__device__ __forceinline__ void lds_wait(float* lds, int idx, unsigned need) {
#pragma unroll 1
  for (unsigned spins = 0; spins < (SPIN_CAP << 6); ++spins) {
    if (__hip_atomic_load(reinterpret_cast<unsigned*>(lds) + L_CNT + idx, __ATOMIC_RELAXED, __HIP_MEMORY_SCOPE_WORKGROUP) >= need) break;
    __builtin_amdgcn_s_sleep(1);
  }
  asm volatile("" ::: "memory");
}

__device__ __forceinline__ void lds_wait_read8(float* lds, int idx, unsigned need, const f32x4* src, f32x4 (&t)[8]) {
#pragma unroll 1
  for (unsigned spins = 0; spins < (SPIN_CAP << 6); ++spins) {
    asm volatile("" ::: "memory");
    const unsigned cnt = __hip_atomic_load(reinterpret_cast<unsigned*>(lds) + L_CNT + idx, __ATOMIC_RELAXED, __HIP_MEMORY_SCOPE_WORKGROUP);
    asm volatile("" ::: "memory");
    SB();
#pragma unroll
    for (int i = 0; i < 8; ++i) t[i] = src[i * 64];
    SB();
    if ((unsigned)__builtin_amdgcn_readfirstlane(cnt) >= need) break;
  }
}
__global__ void __launch_bounds__(NTHR, 2)
fused_decode_kernel(const uint16_t*, const uint16_t*, const uint16_t*, const uint16_t*, const uint16_t*,
                    const uint16_t*, const uint16_t*, const uint16_t*, const uint16_t*, const uint16_t*,
                    uint16_t*, unsigned char*
                    ) {
  __shared__ __attribute__((aligned(16))) float lds[L_FLOATS];
  typedef u64 u64x8 __attribute__((ext_vector_type(8)));
  typedef u64 u64x4 __attribute__((ext_vector_type(4)));
  u64x8 ka; u64x4 kb;
  asm volatile("s_load_dwordx16 %0, %2, 0x0\n\ts_load_dwordx8 %1, %2, 0x40" : "=&s"(ka), "=&s"(kb) : "s"((u64)__builtin_amdgcn_kernarg_segment_ptr()));
  SB();
  const int bid = blockIdx.x, slot = bid >> 3, grp = (bid & 7) * 4 + (slot >> 3), mem = slot & 7;
  const int tid = threadIdx.x, lane = tid & 63, w = __builtin_amdgcn_readfirstlane(tid >> 6);
  const int g = lane >> 4, c = lane & 15;
  const rsrc_t rcode = mk_rsrc((const void*)(__builtin_amdgcn_s_getpc() & ~255ull), CODE_KB * 1024);
  v4u cw = ld16<0>(rcode, (w & 6) == 2 ? lane * 16 : OOB, ((slot + (w & 1) * 13) & 31) * 1024);
  SB();
  constexpr unsigned S = S_FP, SW = (unsigned)(S_FP * W_SCALE), GAP = GAP_FP, SHM = SHIFT_MUL, WQS = (unsigned)(WQ_STEP * S_FP), WOFF = WOFF_TICKS << 8, FLG = DFLG, SHA = SHIFT_A, T_UNIT0 = (unsigned)(UNIT0_SLOTS * S_FP), LEAD = (unsigned)(LEAD_TICKS), XSH = XSHIFT, PDEL = PDELAY << 8, PDEL2 = PDELAY2 << 8, GAP1 = (unsigned)(GAP1_SLOTS * S_FP), CMAX = CATCH_MAX, CMIN = CATCH_MIN, LNOM = LAT_NOM;
#define TR(L) do {} while (0)
  const int hc = (grp + XSH) & 31;
  constexpr unsigned W_BYTES = DM * NH * DH * 2, A_BYTES = 2u * HKVA * SK * DH * 2, B_BYTES = 2u * NH * SK * DH * 2;
  const int qbr = bid & 1, qcol = (bid & 2) * 16 + slot, qrh = (bid >> 2) & 1;
  int voQ = (9 * g + (c >> 3)) * (NH * DH * 2) + (c & 7) * 16, voQ4 = c >= 8 ? OOB : voQ, soQ = (288 * qrh + 36 * w) * (NH * DH * 2) + qcol * 128;
  int xo = (((c >> 3) * DM + 288 * qrh + 36 * w + 9 * g) * 2) & ~3, vot = w == 0 ? ((lane * 9) >> 1) * (NH * DH * 2) : OOB;
  asm volatile("s_waitcnt lgkmcnt(0)" : "+s"(ka), "+s"(kb), "+v"(voQ), "+v"(voQ4), "+v"(xo), "+v"(vot), "+s"(soQ));
  const uint16_t *ax = (const uint16_t*)ka[0], *ak = (const uint16_t*)ka[1], *av = (const uint16_t*)ka[2], *awq = (const uint16_t*)ka[3], *awo = (const uint16_t*)ka[4];
  const uint16_t *bx = (const uint16_t*)ka[5], *bk = (const uint16_t*)ka[6], *bv = (const uint16_t*)ka[7], *bwq = (const uint16_t*)kb[0], *bwo = (const uint16_t*)kb[1];
  uint16_t* out = (uint16_t*)kb[2];
  unsigned char* ws = (unsigned char*)kb[3];
  const rsrc_t rws = mk_rsrc(ws, WS_END);
  v4u Wq[5], xa; unsigned xb;
  const rsrc_t rq = mk_rsrc(qbr ? awq : bwq, W_BYTES), rx = mk_rsrc(qbr ? ax : bx, 2 * DM * 2);
  const unsigned oA = (unsigned)((((grp & 1) * HKVA + (grp >> 1)) << 21) + (SK - WIN) * DH * 2);
  xa = ld16<AUX_NT>(rx, xo, 0);
  xb = __builtin_amdgcn_raw_buffer_load_b32(rx, xo + 16, 0, AUX_NT);
  const unsigned tw = __builtin_amdgcn_raw_buffer_load_b32(rq, vot, soQ, AUX_NT);
  SB();
  unsigned ts = (unsigned)__builtin_amdgcn_s_memrealtime();
  if (w >= 6) {
    unsigned d0, d1;
    asm volatile("s_load_dword %0, %2, 0x0\n\ts_load_dword %1, %3, %4\n\ts_waitcnt lgkmcnt(0)"
                 : "=&s"(d0), "=&s"(d1) : "s"(ws), "s"(w == 6 ? (const unsigned char*)ak : (const unsigned char*)bk), "s"(w == 6 ? oA : (unsigned)grp << 21) : "memory");
  }
  if (w) wait_until(ts, (unsigned)w * WQS);
#pragma unroll
  for (int i = 0; i < 5; ++i) Wq[i] = ld16<AUX_NT>(rq, i == 4 ? voQ4 : voQ, soQ + i * 2 * (NH * DH * 2));
  SB();
  asm volatile("" :: "v"(tw));
  if (w == 0) {
    const unsigned t1 = (unsigned)__builtin_amdgcn_s_memrealtime() - LNOM;
    reinterpret_cast<unsigned*>(lds)[L_NB + 12] = (int)(t1 - ts) > 0 ? t1 : ts;
    STAMP(14);
  }
  u64 epoch;
  asm volatile("s_load_dwordx2 %0, %1, 0x0\n\ts_waitcnt lgkmcnt(0)" : "=s"(epoch) : "s"(ws) : "memory");
  const u64 tag = epoch + 0x0040100401ull;
  STAMP(9);
  if (tid < 16) reinterpret_cast<unsigned*>(lds)[L_CNT + tid] = 0u;
  const int bA = grp & 1, hA = grp & ~1;
  v4u mask;
#pragma unroll
  for (int d = 0; d < 4; ++d) mask[d] = (((c & 7) >> 1) == d) ? ((c & 1) ? 0xffff0000u : 0x0000ffffu) : 0u;
  __syncthreads();
  {
    ts = __builtin_amdgcn_readfirstlane(reinterpret_cast<const unsigned*>(lds)[L_NB + 12]);
    const unsigned t1 = (unsigned)__builtin_amdgcn_s_memrealtime() - (T_UNIT0 >> 8);
    if ((int)(t1 - ts) > 0) ts = t1;
  }
  if (w == 7) __builtin_amdgcn_raw_buffer_store_b32(((unsigned)tag << 10) | (ts & 1023u), rws, lane == 0 ? 0 : OOB, (int)WS_CK + bid * 4, AUX_SC1);
  STAMP(11);
  int voK = ((lane >> 2) & 3) * 256 + (4 * g + (lane & 3)) * 16;
  const int voV = lane * 16;
  const int jB = w * 8 + mem + grp * SHM, jA = w * 8 + mem + (bA * HKVA + (grp >> 1)) * SHA;
  const int baseA = (int)oA;
  auto offA = [&](int r) { return baseA + (((r * 64 + jA) & 127) << 12); };
  auto offB = [&](int r) { return (((r >> 3) * NH + grp) << 21) + ((((r & 7) * 64 + jB) & 511) << 12); };
  v4u R[6][4];
  unsigned behind = 0, cstep = 0;
  unsigned lim = T_UNIT0 + (unsigned)w * S;
#define REQ(SL, RS, VO, O) do { behind = behind > cstep ? behind - cstep : 0u; wait_until(ts, lim + behind); const int o_ = (O); asm volatile("" : "+v"(VO)); \
    _Pragma("unroll") for (int a_ = 0; a_ < 4; ++a_) R[SL][a_] = ld16<AUX_NT>(RS, (VO) + a_ * 1024, o_); \
    SB(); TR(lim); \
    lim += 8 * S; } while (0)
  float m = -INFINITY, l = 0.f;
  float2 nb = make_float2(0.f, 0.f);
  f32x4 acc = {0.f, 0.f, 0.f, 0.f};
  v2u* const scb = reinterpret_cast<v2u*>(lds + L_SC) + w * 18 * 64 + lane;
  const unsigned pbase = WS_OB + (unsigned)hc * 8192 + w * 1024 + c * 64 + g * 16;
  v4u gp[1] = {v4u{0u, 0u, 0u, 0u}};
  f32x4* const ob = reinterpret_cast<f32x4*>(lds + L_OB) + lane;
  auto deposit = [&](int ph) {
    ob[ph * 512 + w * 64] = acc;
    acc = f32x4{0.f, 0.f, 0.f, 0.f};
    (void)lds_arrive(lds, 1 + ph, lane);
  };
  const float* qsrc = lds + L_QS + (4 * g + (c & 3)) * 4;
  v4u qB;
  {
    const rsrc_t rka = mk_rsrc(ak, A_BYTES), rkb = mk_rsrc(bk, B_BYTES);
    REQ(4, rka, voK, offA(0));
    {
      const unsigned xr[5] = {xa[0], xa[1], xa[2], xa[3], xb};
      const unsigned s0 = (g & 1) ? 0x03020302u : 0x01000100u, s1 = (g & 1) ? 0x05040504u : 0x03020302u;
      f32x4 qa = {0.f, 0.f, 0.f, 0.f}, qb = {0.f, 0.f, 0.f, 0.f};
#pragma unroll
      for (int i = 0; i < 5; ++i) {
        qa = mfma(diag(__builtin_amdgcn_perm(xr[i], xr[i], s0), mask), Wq[i], qa);
        if (i < 4) qb = mfma(diag(__builtin_amdgcn_perm(xr[i + 1], xr[i], s1), mask), Wq[i], qb);
      }
#pragma unroll
      for (int i = 0; i < 4; ++i) qa[i] += dppf<ROR8>(qb[i]);
      if (c < 8) reinterpret_cast<f32x4*>(lds + L_QH)[w * 32 + (g >> 1) * 16 + c * 2 + (g & 1)] = qa;
      asm volatile("" :: "v"(cw));
      if (lds_arrive(lds, 6, lane) == 7u) {
        const f32x4* t = reinterpret_cast<const f32x4*>(lds + L_QH) + (lane & 31);
        f32x4 sq = ((t[0] + t[32]) + (t[64] + t[96])) + ((t[128] + t[160]) + (t[192] + t[224]));
        if (lane >= 32) {
#pragma unroll
          for (int i = 0; i < 4; ++i) sq[i] -= __uint_as_float((__float_as_uint(sq[i]) + 0x200u) & ~0x3ffu);
        }
        publish(rws, (lane >> 5) * 131072 + ((lane >> 4) & 1) * 1024 + (lane & 15) * 16,
                (int)(WS_QP + (unsigned)(qbr * 32 + (qcol >> 1)) * 2048 + qrh * 512 + (qcol & 1) * 256), sq, tag);
        STAMP(10);
      }
      SB();
    }
    REQ(5, rka, voK, offA(1));
    if (w == mem) {
      const unsigned char* tp = (const unsigned char*)((const __attribute__((address_space(4))) u64*)(u64)__builtin_amdgcn_kernarg_segment_ptr())[(0xAA947261u >> (4 * w)) & 15];
      const unsigned to0 = w >= 6 ? 0u : w >= 4 ? (unsigned)hc * (128 * DM * 2) : (w & 1) ? (unsigned)grp << 21 : oA;
      const unsigned to1 = to0 + (w >= 6 ? 0u : w >= 4 ? 127u * DM * 2 : (w & 1) ? (unsigned)NH * SK * DH * 2 : 0u);
      unsigned d0, d1, d2, d3;
      const unsigned tm = (w & 6) == 4 ? 48u * 1024 : 0u;
      asm volatile("s_load_dword %0, %4, %5\n\ts_load_dword %1, %4, %6\n\ts_load_dword %2, %4, %7\n\ts_load_dword %3, %4, %8\n\ts_waitcnt lgkmcnt(0)"
                   : "=&s"(d0), "=&s"(d1), "=&s"(d2), "=&s"(d3) : "s"(tp), "s"(to0), "s"(to1), "s"(to0 + tm), "s"(to1 - tm) : "memory");
    }
    lim += GAP1;
    REQ(0, rkb, voK, offB(0));
    if (w == 4) {
      v4u gq[1];
      load_granules<1>(rws, WS_CK + lane * 16, 0, gq);
      float d = -512.f, sm = 0.f;
#pragma unroll
      for (int i = 0; i < 4; ++i) {
        const float di = ((gq[0][i] ^ ((unsigned)tag << 10)) < 1024u) ? (float)((int)((gq[0][i] - ts) << 22) >> 22) : 0.f;
        d = fmaxf(d, di); sm += di;
      }
      d = r8_max(d); d = xg_max(fmaxf(d, dppf<ROR8>(d)));
      sm = r8_sum(sm); sm = xg_sum(sm + dppf<ROR8>(sm)) * (1.f / 256.f);
      reinterpret_cast<unsigned*>(lds)[L_NB + 13] = ts + (unsigned)(int)__builtin_floorf(d - fminf(fmaxf(d - sm - (float)CMIN, 0.f), (float)CMAX)) - LEAD;
      (void)lds_arrive(lds, 7, lane);
    }
    SB();
    REQ(1, rkb, voK, offB(1));
    const unsigned go = w < 2 ? WS_QP + (unsigned)(grp * 2 + w) * 1024 + lane * 16 : WS_QP + (unsigned)((32 + hA + w - 2) * 2 + bA) * 1024 + lane * 16;
    v4u gq[2];
    auto take_q = [&]() {
      STAMP(12);
      float t[4];
#pragma unroll
      for (int i = 0; i < 4; ++i) t[i] = x32_sum(untag(gq[0][i]) + untag(gq[1][i]));
      if (lane < 32) *reinterpret_cast<v2u*>(lds + L_QS + w * 64 + lane * 2) = v2u{pk_bf16(t[0], t[1]), pk_bf16(t[2], t[3])};
      (void)lds_arrive(lds, 0, lane);
    };
    bool qlate = false; int early;
    if (w < 4) { qlate = !poll_granules<2>(rws, go, 131072, tag, gq, 1u); if (!qlate) take_q(); }
    SB();
    {
      lds_wait(lds, 7, 1u);
      const unsigned tc = __builtin_amdgcn_readfirstlane(reinterpret_cast<const unsigned*>(lds)[L_NB + 13]);
      behind = (int)(ts - tc) > 0 ? (ts - tc) << 8 : 0u;
      cstep = min(behind, CMAX << 8) / 31 + 1;
      ts = tc;
    }
    REQ(2, rkb, voK, offB(2));
    if (qlate) { qlate = !poll_granules<2>(rws, go, 131072, tag, gq, 1u); if (!qlate) take_q(); }
    early = __hip_atomic_load(reinterpret_cast<unsigned*>(lds) + L_CNT, __ATOMIC_RELAXED, __HIP_MEMORY_SCOPE_WORKGROUP) >= 4u;
    early = __builtin_amdgcn_readfirstlane(early);
    asm volatile("" ::: "memory");
    auto a_units = [&]() {
      qB = *reinterpret_cast<const v4u*>(qsrc + 128 + (c >> 3) * 64);
      k_step(R[4], qB, m, l, scb);
      k_step(R[5], qB, m, l, scb + 64);
    };
    if (early) a_units();
    REQ(3, rkb, voK, offB(3));
    if (!early) {
      if (qlate) { (void)poll_granules<2>(rws, go, 131072, tag, gq); take_q(); }
      lds_wait(lds, 0, 4u);
      a_units();
    }
  }
  STAMP(1);
  auto end_k_set = [&](int set) {
    const float mw = xg_max(m);
    const float lw = xg_sum(l * __builtin_amdgcn_exp2f((m - mw) * LOG2E));
    f32x4* ml = reinterpret_cast<f32x4*>(lds + L_QH) + 256 + set * 8;
    const f32x4 rec = {mw, lw, dppf<ROR8>(mw), dppf<ROR8>(lw)};
    if (lane == 0) ml[w] = rec;
    if (lds_arrive(lds, 12 + set, lane) == 7u)
      publish(rws, lane < 8 ? lane * 16 : OOB, (int)(WS_ML + (unsigned)(grp * 3 + set) * 1024 + mem * 128), ml[lane & 7], tag);
    m = -INFINITY; l = 0.f;
    qB = *reinterpret_cast<const v4u*>(qsrc + (set ? 64 : 0));
  };
  end_k_set(0);
  const int voR = (g + 8 * (c >> 3)) * 1152 + (c & 7) * 16, soW = (hc * 128 + 32 * (w >> 1) + 4 * (w & 1)) * 1152;
  v4u WX[2] = {v4u{0u, 0u, 0u, 0u}, v4u{0u, 0u, 0u, 0u}};
#pragma unroll 1
  for (int it = 0; it < 7; ++it) {
    const bool isk = it < 4, rv = it >= 3;
    const rsrc_t rs = mk_rsrc(rv ? bv : bk, B_BYTES);
    int vo = rv ? voV : voK;
    const int r0 = (4 * it + 4) & 15;
    v2u* sc = scb + (2 + ((4 * it) & 15)) * 64;
    const bool stat = (it == 1 || it == 2 || it == 4) && w == it + 1;
    const unsigned soff = WS_ML + (unsigned)(grp * 3 + (it >> 1)) * 1024 + lane * 16;
    if (it == 3) {
      const rsrc_t rva = mk_rsrc(av, A_BYTES);
      lim += GAP;
      REQ(4, rva, vo, offA(0));
      REQ(5, rva, vo, offA(1));
      if (mem < 2) {
        const rsrc_t rwx = mk_rsrc(mem ? awo : bwo, W_BYTES);
        WX[0] = ld16<AUX_NT>(rwx, voR, soW + 1024); WX[1] = ld16<AUX_NT>(rwx, voR, soW + 1024 + 16 * 1152);
        SB();
      }
      lim += GAP + S;
    }
    if (isk) k_step(R[0], qB, m, l, sc); else v_step(R[0], mask, nb, acc, sc);
    REQ(0, rs, vo, offB(r0));
    if (isk) k_step(R[1], qB, m, l, sc + 64); else v_step(R[1], mask, nb, acc, sc + 64);
    REQ(1, rs, vo, offB(r0 + 1));
    if (isk) k_step(R[2], qB, m, l, sc + 128); else v_step(R[2], mask, nb, acc, sc + 128);
    REQ(2, rs, vo, offB(r0 + 2));
    if (stat) { load_granules<1>(rws, soff, 0, gp); SB(); }
    if (it == 5 && w == 3) { cw = ld16<0>(rcode, lane * 16, (slot + 12) * 1024); SB(); }
    if (it == 3) {
      lds_wait(lds, 8, 1u);
      nb = reinterpret_cast<const float2*>(lds + L_NB)[c >> 3];
      v_step(R[4], mask, nb, acc, scb);
      v_step(R[5], mask, nb, acc, scb + 64);
    }
    if (isk) k_step(R[3], qB, m, l, sc + 192); else v_step(R[3], mask, nb, acc, sc + 192);
    REQ(3, rs, vo, offB(r0 + 3));
    if (stat) {
      (void)check_granules<1>(rws, soff, 0, tag, gp, SPIN_CAP);
#pragma unroll
      for (int h = 0; h < 2; ++h) {
        const float mw = untag(gp[0][2 * h]);
        float M = r8_max(mw); M = xg_max(fmaxf(M, dppf<ROR8>(M)));
        float L = r8_sum(untag(gp[0][2 * h + 1]) * __builtin_amdgcn_exp2f((mw - M) * LOG2E));
        L = xg_sum(L + dppf<ROR8>(L));
        if (lane == 0) reinterpret_cast<float2*>(lds + L_NB)[(it >> 1) * 2 + h] = make_float2(M, -__builtin_amdgcn_logf(L));
      }
      (void)lds_arrive(lds, 8 + (it >> 1), lane);
      SB();
    }
    if (it & 1) {
      if (isk) end_k_set((it + 1) >> 1);
      if (it >= 3) {
        const int ph = (it - 3) >> 1;
        deposit(ph);
        lds_wait(lds, 9 + ph, 1u);
        nb = reinterpret_cast<const float2*>(lds + L_NB)[2 + 2 * ph];
      }
      SB();
    }
  }
#undef REQ
#pragma unroll
  for (int s = 0; s < 3; ++s) v_step(R[s], mask, nb, acc, scb + (14 + s) * 64);
  lim -= (unsigned)w * S;
  asm volatile("" :: "v"(cw));
  v4u WA[2], WB[2];
  const rsrc_t rwa = mk_rsrc(awo, W_BYTES), rwb = mk_rsrc(bwo, W_BYTES);
  wait_until(ts, lim + WOFF + (unsigned)(w * 8) * SW / 16);
  WA[0] = ld16<AUX_NT>(rwa, voR, soW + mem * 128); WA[1] = ld16<AUX_NT>(rwa, voR, soW + mem * 128 + 16 * 1152);
  SB();
  {
    const int ph = w == 7 ? 2 : w;
    const bool on = w < 2 || w == 7;
    int pvo = (on && (c < 8 || ph == 0)) ? (c >> 3) * 8192 + (2 * g + ((c >> 2) & 1)) * 1024 + (c & 3) * 16 : OOB;
    int pso = (int)(WS_OB + (unsigned)(ph == 0 ? 32 + hA : grp) * 8192 + (ph == 0 ? bA * 8 + mem : (ph - 1) * 8 + mem) * 64);
    const f32x4* src = ob + (on ? ph : 0) * 512;
    asm volatile("" : "+v"(pvo), "+s"(pso));
    SB();
    v_step(R[3], mask, nb, acc, scb + 17 * 64);
    STAMP(2);
    if (w < 7) deposit(2);
    if (on) {
      f32x4 t[8];
      lds_wait_read8(lds, 1 + ph, w == 7 ? 7u : 8u, src, t);
      if (w == 7) STAMP(3);
      if (w < 7) acc = t[7];
      acc = ((t[0] + t[1]) + (t[2] + t[3])) + ((t[4] + t[5]) + (t[6] + acc));
    }
    publish(rws, pvo, pso, acc, tag);
    if (w == 7) STAMP(4); else if (on) STAMP(15);
  }
  SB();
  wait_until(ts, lim + WOFF + (unsigned)(64 + w * 8) * SW / 16);
  WB[0] = ld16<AUX_NT>(rwb, voR, soW + mem * 128); WB[1] = ld16<AUX_NT>(rwb, voR, soW + mem * 128 + 16 * 1152);
  SB();
  f32x4* const yw = reinterpret_cast<f32x4*>(lds + L_YB) + w * 64 + lane;
  int ydst = ((g & 1) * 18 + 2 * ((lane & 32) ? 8 : mem) + (c >> 3)) * 4096 + (c & 7) * 16, yoff = (g >> 1) * 512 + ((g & 1) * 2 + (c & 1)) * 16 + (c >> 1);
  asm volatile("" : "+v"(ydst), "+v"(yoff));
  {
    v4u ge[2];
    wait_until(ts, lim + PDEL);
    (void)poll_granules<2>(rws, pbase, 32 * 8192, tag, ge);
    STAMP(5);
    float t[8];
#pragma unroll
    for (int i = 0; i < 8; ++i) t[i] = untag(ge[i >> 2][i & 3]);
    asm volatile("s_nop 1\n\t"
                 "v_add_f32_dpp %0, %0, %0 quad_perm:[1,0,3,2] row_mask:0xf bank_mask:0xf\n\tv_add_f32_dpp %1, %1, %1 quad_perm:[1,0,3,2] row_mask:0xf bank_mask:0xf\n\t"
                 "v_add_f32_dpp %2, %2, %2 quad_perm:[1,0,3,2] row_mask:0xf bank_mask:0xf\n\tv_add_f32_dpp %3, %3, %3 quad_perm:[1,0,3,2] row_mask:0xf bank_mask:0xf\n\t"
                 "v_add_f32_dpp %4, %4, %4 quad_perm:[1,0,3,2] row_mask:0xf bank_mask:0xf\n\tv_add_f32_dpp %5, %5, %5 quad_perm:[1,0,3,2] row_mask:0xf bank_mask:0xf\n\t"
                 "v_add_f32_dpp %6, %6, %6 quad_perm:[1,0,3,2] row_mask:0xf bank_mask:0xf\n\tv_add_f32_dpp %7, %7, %7 quad_perm:[1,0,3,2] row_mask:0xf bank_mask:0xf\n\t"
                 "v_add_f32_dpp %0, %0, %0 quad_perm:[2,3,0,1] row_mask:0xf bank_mask:0xf\n\tv_add_f32_dpp %1, %1, %1 quad_perm:[2,3,0,1] row_mask:0xf bank_mask:0xf\n\t"
                 "v_add_f32_dpp %2, %2, %2 quad_perm:[2,3,0,1] row_mask:0xf bank_mask:0xf\n\tv_add_f32_dpp %3, %3, %3 quad_perm:[2,3,0,1] row_mask:0xf bank_mask:0xf\n\t"
                 "v_add_f32_dpp %4, %4, %4 quad_perm:[2,3,0,1] row_mask:0xf bank_mask:0xf\n\tv_add_f32_dpp %5, %5, %5 quad_perm:[2,3,0,1] row_mask:0xf bank_mask:0xf\n\t"
                 "v_add_f32_dpp %6, %6, %6 quad_perm:[2,3,0,1] row_mask:0xf bank_mask:0xf\n\tv_add_f32_dpp %7, %7, %7 quad_perm:[2,3,0,1] row_mask:0xf bank_mask:0xf\n\t"
                 "v_add_f32_dpp %0, %0, %0 row_half_mirror row_mask:0xf bank_mask:0xf\n\tv_add_f32_dpp %1, %1, %1 row_half_mirror row_mask:0xf bank_mask:0xf\n\t"
                 "v_add_f32_dpp %2, %2, %2 row_half_mirror row_mask:0xf bank_mask:0xf\n\tv_add_f32_dpp %3, %3, %3 row_half_mirror row_mask:0xf bank_mask:0xf\n\t"
                 "v_add_f32_dpp %4, %4, %4 row_half_mirror row_mask:0xf bank_mask:0xf\n\tv_add_f32_dpp %5, %5, %5 row_half_mirror row_mask:0xf bank_mask:0xf\n\t"
                 "v_add_f32_dpp %6, %6, %6 row_half_mirror row_mask:0xf bank_mask:0xf\n\tv_add_f32_dpp %7, %7, %7 row_half_mirror row_mask:0xf bank_mask:0xf\n\t"
                 : "+v"(t[0]), "+v"(t[1]), "+v"(t[2]), "+v"(t[3]), "+v"(t[4]), "+v"(t[5]), "+v"(t[6]), "+v"(t[7]));
    v4u A[2][4];
#pragma unroll
    for (int i = 0; i < 8; ++i) A[i >> 2][i & 3] = diag(pk_bf16(t[i], t[i]), mask);
    const f32x4 y0 = y_half(WB[0], WB[1], A[0]), y1 = y_half(WA[0], WA[1], A[1]);
    yw[0] = y0; yw[1024] = y1;
    if (mem == 0) yw[512] = y_half(WX[0], WX[1], A[0]);
    if (mem == 1) yw[1024 + 512] = y_half(WX[0], WX[1], A[1]);
    const unsigned old = lds_arrive(lds, 4, lane);
    if (old >= 6u) {
      const int br = 7 - (int)old;
      f32x4 u[8];
      lds_wait_read8(lds, 4, 8u, reinterpret_cast<const f32x4*>(lds + L_YB) + br * 1024 + yoff, u);
      publish(rws, ((lane & 32) && mem != br) ? OOB : ydst, (int)WS_Y + br * 36 * 4096 + hc * 128, ((u[0] + u[1]) + (u[2] + u[3])) + ((u[4] + u[5]) + (u[6] + u[7])), tag);
      STAMP(6);
    }
    if (w == 0 && bid >= 16 && bid < 88) {
      const int rr = bid - 16;
      const int br = rr >= 36, bb = (rr - 36 * br) >= 18, cb = rr - 36 * br - 18 * bb;
      const int off = lane < 8 ? (bb * 2 * DM + (br ? 0 : DM) + cb * 32 + lane * 4) * 2 : OOB;
      const rsrc_t ro = mk_rsrc(out, 2 * 2 * DM * 2);
      unsigned yo = WS_Y + (unsigned)rr * 4096 + lane * 16;
      v4u gv[4];
      wait_until(ts, lim + PDEL2);
      (void)poll_granules<4>(rws, yo, 1024, tag, gv);
      float tt[4];
#pragma unroll
      for (int i = 0; i < 4; ++i) {
        tt[i] = (untag(gv[0][i]) + untag(gv[1][i])) + (untag(gv[2][i]) + untag(gv[3][i]));
        tt[i] += dppf<ROR8>(tt[i]);
        tt[i] = xg_sum(tt[i]);
      }
      __builtin_amdgcn_raw_buffer_store_b64(v2u{pk_bf16(tt[0], tt[1]), pk_bf16(tt[2], tt[3])}, ro, off, 0, 0);
      STAMP(7);
    }
  }
  if (bid == 255 && w == 3) {
    v4u gc[4];
    (void)poll_granules<4>(rws, WS_QP + (unsigned)(lane >> 2) * 2048 + (lane & 3) * 256, 32768, tag, gc);
    __builtin_amdgcn_raw_buffer_store_b64(v2u{(unsigned)tag, (unsigned)(tag >> 32)}, rws, lane == 0 ? (int)WS_EPOCH : OOB, 0, AUX_SC1);
  }
}

extern "C" void kernel_launch(void* const* d_in, const int* in_sizes, int n_in, void* d_out, int out_size, void* d_ws, size_t ws_size,
                              hipStream_t stream) {
  (void)in_sizes; (void)n_in; (void)out_size; (void)ws_size;
  fused_decode_kernel<<<NWG, NTHR, 0, stream>>>(
      (const uint16_t*)d_in[0], (const uint16_t*)d_in[1], (const uint16_t*)d_in[2], (const uint16_t*)d_in[3], (const uint16_t*)d_in[4],
      (const uint16_t*)d_in[5], (const uint16_t*)d_in[6], (const uint16_t*)d_in[7], (const uint16_t*)d_in[8], (const uint16_t*)d_in[9],
      (uint16_t*)d_out, (unsigned char*)d_ws + WS_BASE
      );
}
```
